```python
import torch

def _softmax(x, axis=-1):
    return torch.softmax(x, dim=axis)
def _gelu(x):
    return torch.nn.functional.gelu(x, approximate="tanh")
def _silu(x):
    return torch.nn.functional.silu(x)
def _sigmoid(x):
    return torch.sigmoid(x)
def _layernorm(x, axis=-1, eps=1e-5):
    m = x.mean(dim=axis, keepdim=True)
    v = x.var(dim=axis, keepdim=True, unbiased=False)
    return (x - m) / torch.sqrt(v + eps)
def _scatter_add(tgt, idx, src, axis=0):
    return tgt.clone().index_add_(axis, idx, src)
def _segment_sum(src, seg, n):
    out = torch.zeros((n,) + src.shape[1:], dtype=src.dtype)
    return out.index_add_(0, seg, src)
def _pad(x, pw):
    flat = tuple(v for pair in reversed(pw) for v in pair)
    return torch.nn.functional.pad(x, flat)

kernel_name = 'concat_heads__gqa_block_cached__gqa_block_cached__decode'

def setup_inputs():

    _w = (lambda a: (lambda t: t.to(torch.bfloat16) if t.is_floating_point() else t)(torch.as_tensor(a)))
    def _A_setup():
        B, Sq, D = 1, 1, 640
        Hq, Hkv, Sk, Dh = 32, 32, 32768, 192
        assert Hq % Hkv == 0, (Hq, Hkv)
        return {
            "x": _w((lambda *s: torch.randn(*s, dtype=torch.bfloat16) * 0.8)(B, Sq, D)),
            "k_cache": _w((lambda *s: torch.randn(*s, dtype=torch.bfloat16) * 0.8)(B, Hkv, Sk, Dh)),
            "v_cache": _w((lambda *s: torch.randn(*s, dtype=torch.bfloat16) * 0.05)(B, Hkv, Sk, Dh)),
            "wq": _w((lambda *s: torch.randn(*s, dtype=torch.bfloat16) * 0.8)(D, Hq * Dh) * (D ** -0.5)),
            "wo": _w((lambda *s: torch.randn(*s, dtype=torch.bfloat16) * 0.05)(Hq * Dh, D) * ((Hq * Dh) ** -0.5)),
        }
    _A_in = _A_setup()

    def _B_setup():
        B, Sq, D = 1, 1, 640
        Hq, Hkv, Sk, Dh = 32, 32, 32768, 192
        assert Hq % Hkv == 0, (Hq, Hkv)
        return {
            "x": _w((lambda *s: torch.randn(*s, dtype=torch.bfloat16) * 0.8)(B, Sq, D)),
            "k_cache": _w((lambda *s: torch.randn(*s, dtype=torch.bfloat16) * 0.8)(B, Hkv, Sk, Dh)),
            "v_cache": _w((lambda *s: torch.randn(*s, dtype=torch.bfloat16) * 0.05)(B, Hkv, Sk, Dh)),
            "wq": _w((lambda *s: torch.randn(*s, dtype=torch.bfloat16) * 0.8)(D, Hq * Dh) * (D ** -0.5)),
            "wo": _w((lambda *s: torch.randn(*s, dtype=torch.bfloat16) * 0.05)(Hq * Dh, D) * ((Hq * Dh) ** -0.5)),
        }
    _B_in = _B_setup()

    return {
        "A_x": _A_in["x"],
        "A_k_cache": _A_in["k_cache"],
        "A_v_cache": _A_in["v_cache"],
        "A_wq": _A_in["wq"],
        "A_wo": _A_in["wo"],
        "B_x": _B_in["x"],
        "B_k_cache": _B_in["k_cache"],
        "B_v_cache": _B_in["v_cache"],
        "B_wq": _B_in["wq"],
        "B_wo": _B_in["wo"],
    }

def reference(A_x, A_k_cache, A_v_cache, A_wq, A_wo, B_x, B_k_cache, B_v_cache, B_wq, B_wo):
    def _A_ref(x, k_cache, v_cache, wq, wo):
        B, Sq, D = x.shape
        Hq, Dh = 32, 192
        Hkv = k_cache.shape[1]
        rep = Hq // Hkv
        q = (lambda x, *d: x.permute(*d))((x @ wq).reshape(B, Sq, Hq, Dh), 0, 2, 1, 3)
        k = (lambda x, n, axis: torch.repeat_interleave(x, n, dim=axis))(k_cache, rep, axis=1)
        v = (lambda x, n, axis: torch.repeat_interleave(x, n, dim=axis))(v_cache, rep, axis=1)
        scores = torch.einsum("bhqd,bhkd->bhqk", q, k) * (Dh ** -0.5)
        p = _softmax(scores, axis=-1)
        o = torch.einsum("bhqk,bhkd->bhqd", p, v)
        return (lambda x, *d: x.permute(*d))(o, 0, 2, 1, 3).reshape(B, Sq, Hq * Dh) @ wo

    def _B_ref(x, k_cache, v_cache, wq, wo):
        B, Sq, D = x.shape
        Hq, Dh = 32, 192
        Hkv = k_cache.shape[1]
        rep = Hq // Hkv
        q = (lambda x, *d: x.permute(*d))((x @ wq).reshape(B, Sq, Hq, Dh), 0, 2, 1, 3)
        k = (lambda x, n, axis: torch.repeat_interleave(x, n, dim=axis))(k_cache, rep, axis=1)
        v = (lambda x, n, axis: torch.repeat_interleave(x, n, dim=axis))(v_cache, rep, axis=1)
        scores = torch.einsum("bhqd,bhkd->bhqk", q, k) * (Dh ** -0.5)
        p = _softmax(scores, axis=-1)
        o = torch.einsum("bhqk,bhkd->bhqd", p, v)
        return (lambda x, *d: x.permute(*d))(o, 0, 2, 1, 3).reshape(B, Sq, Hq * Dh) @ wo

    a = _A_ref(A_x, A_k_cache, A_v_cache, A_wq, A_wo)
    b = _B_ref(B_x, B_k_cache, B_v_cache, B_wq, B_wo)
    return torch.concatenate([a, b], axis=-1)
```

```cpp
#include <hip/hip_runtime.h>
#include <hip/hip_bf16.h>

#define GAS __attribute__((address_space(1)))
#define LAS __attribute__((address_space(3)))
typedef unsigned v4u __attribute__((ext_vector_type(4)));
typedef unsigned v2u __attribute__((ext_vector_type(2)));
typedef float f32x4 __attribute__((ext_vector_type(4)));
typedef float f32x2 __attribute__((ext_vector_type(2)));
typedef __bf16 bf16x8 __attribute__((ext_vector_type(8)));
typedef short s16x4 __attribute__((ext_vector_type(4)));

#define K_NSUB 1
#define K_INFL 18
#define K_NCONS 2

constexpr int DM = 640, NH = 32, DH = 192, SK = 32768, HD = NH * DH;
constexpr int NBH = 2 * NH;
constexpr int NWG = 256;
constexpr int SUB = 128;
constexpr int NSUB = K_NSUB;
constexpr int NP = SK / (SUB * NSUB);
constexpr int NSTEP = NBH * NP / NWG;
constexpr int ROWB = DH * 2;
constexpr int SLOT_BYTES = 32 * ROWB;
constexpr int NSLOT = 12;
constexpr int NFILL = NSTEP * NSUB * 8;
constexpr int INFL = K_INFL;
constexpr int NCONS = K_NCONS;
constexpr int RING_BYTES = NSLOT * SLOT_BYTES;
constexpr int FULL_OFF = RING_BYTES, FREE_OFF = RING_BYTES + 64, LDS_BYTES = RING_BYTES + 128;
constexpr float C2 = 0.07216878364870322f * 1.4426950408889634f;
constexpr unsigned SPIN_CAP = 1u << 22;

constexpr size_t WS_Q = 0;
constexpr size_t WS_PML = 65536;
constexpr size_t WS_PO = WS_PML + (size_t)NBH * 256 * 8;
constexpr size_t WS_OP = WS_PO + (size_t)NBH * 256 * DH * 4;
constexpr size_t WS_END = WS_OP + (size_t)256 * DM * 4;

__device__ __forceinline__ float bflo(unsigned u) { return __builtin_bit_cast(float, u << 16); }
__device__ __forceinline__ float bfhi(unsigned u) { return __builtin_bit_cast(float, u & 0xffff0000u); }
__device__ __forceinline__ unsigned cvt_pk_bf16(float lo, float hi) {
    typedef __bf16 b2 __attribute__((ext_vector_type(2)));
    b2 r = {(__bf16)lo, (__bf16)hi};
    return __builtin_bit_cast(unsigned, r);
}
__device__ __forceinline__ f32x4 mfma16(v4u a, v4u b, f32x4 c) {
    return __builtin_amdgcn_mfma_f32_16x16x32_bf16(__builtin_bit_cast(bf16x8, a), __builtin_bit_cast(bf16x8, b), c, 0, 0, 0);
}
__device__ __forceinline__ float xor16_max(float x) { auto s = __builtin_amdgcn_permlane16_swap(__float_as_uint(x), __float_as_uint(x), false, false); return fmaxf(__uint_as_float(s[0]), __uint_as_float(s[1])); }
__device__ __forceinline__ float xor32_max(float x) { auto s = __builtin_amdgcn_permlane32_swap(__float_as_uint(x), __float_as_uint(x), false, false); return fmaxf(__uint_as_float(s[0]), __uint_as_float(s[1])); }
__device__ __forceinline__ float xor16_sum(float x) { auto s = __builtin_amdgcn_permlane16_swap(__float_as_uint(x), __float_as_uint(x), false, false); return __uint_as_float(s[0]) + __uint_as_float(s[1]); }
__device__ __forceinline__ float xor32_sum(float x) { auto s = __builtin_amdgcn_permlane32_swap(__float_as_uint(x), __float_as_uint(x), false, false); return __uint_as_float(s[0]) + __uint_as_float(s[1]); }

__device__ __forceinline__ void lds_st(unsigned addr, unsigned v) { asm volatile("ds_write_b32 %0, %1" : : "v"(addr), "v"(v) : "memory"); }
__device__ __forceinline__ unsigned lds_ld(unsigned addr) { unsigned v; asm volatile("ds_read_b32 %0, %1\n\ts_waitcnt lgkmcnt(0)" : "=&v"(v) : "v"(addr) : "memory"); return __builtin_amdgcn_readfirstlane(v); }
__device__ __forceinline__ void glds3(unsigned voff, unsigned long long sbase, unsigned ldsdst) {
    unsigned keep;
    asm volatile("s_mov_b32 %0, m0\n\ts_mov_b32 m0, %3\n\ts_nop 0\n\t"
                 "global_load_lds_dwordx4 %1, %2 nt\n\t"
                 "global_load_lds_dwordx4 %1, %2 offset:1024 nt\n\t"
                 "global_load_lds_dwordx4 %1, %2 offset:2048 nt\n\t"
                 "s_mov_b32 m0, %0"
                 : "=&s"(keep) : "v"(voff), "s"(sbase), "s"(ldsdst) : "memory");
}

__global__ void __launch_bounds__(256) k_qproj(const unsigned short* __restrict__ xa, const unsigned short* __restrict__ wqa,
                                               const unsigned short* __restrict__ xb, const unsigned short* __restrict__ wqb, unsigned char* __restrict__ ws) {
    __shared__ float red[32][64 + 1];
    __shared__ float xs[DM];
    const int b = blockIdx.x / 96, cb = blockIdx.x % 96, tid = threadIdx.x;
    const unsigned short* x = b ? xb : xa; const unsigned short* wq = b ? wqb : wqa;
    const int rg = tid >> 3, c8 = tid & 7;
    v4u w[20];
#pragma unroll
    for (int i = 0; i < 20; ++i) w[i] = __builtin_nontemporal_load((const GAS v4u*)(wq + (size_t)(rg + 32 * i) * HD + cb * 64 + c8 * 8));
    for (int i = tid; i < DM; i += 256) xs[i] = bflo(x[i]);
    __syncthreads();
    float a[8] = {0.f, 0.f, 0.f, 0.f, 0.f, 0.f, 0.f, 0.f};
#pragma unroll
    for (int i = 0; i < 20; ++i) { const float xv = xs[rg + 32 * i];
#pragma unroll
        for (int k = 0; k < 4; ++k) { a[2 * k] = fmaf(xv, bflo(w[i][k]), a[2 * k]); a[2 * k + 1] = fmaf(xv, bfhi(w[i][k]), a[2 * k + 1]); } }
#pragma unroll
    for (int k = 0; k < 8; ++k) red[rg][c8 * 8 + k] = a[k];
    __syncthreads();
    if (tid < 64) { float s = 0.f;
#pragma unroll
        for (int r = 0; r < 32; ++r) s += red[r][tid];
        ((__bf16*)(ws + WS_Q))[b * HD + cb * 64 + tid] = (__bf16)s; }
}

struct StreamArgs { const unsigned char* k[2]; const unsigned char* v[2]; unsigned char* ws; };

__global__ void __launch_bounds__((1 + NCONS) * 64, 1) k_stream(StreamArgs a) {
    extern __shared__ __attribute__((aligned(16))) unsigned char lds[];
    const int tid = threadIdx.x, lane = tid & 63, wave = __builtin_amdgcn_readfirstlane(tid >> 6);
    const int wg = blockIdx.x;
    const unsigned lds0 = (unsigned)(uintptr_t)lds;
    if (tid < 32) lds_st(lds0 + FULL_OFF + tid * 4, 0u);
    asm volatile("s_waitcnt lgkmcnt(0)\n\ts_barrier" ::: "memory");

    if (wave == 0) {
        const unsigned voff = lane * 16;
        unsigned pub = 0, slot = 0;
        for (int s = 0; s < NSTEP; ++s) {
            const int fb = s * NWG + wg, bh = fb / NP, key0 = (fb % NP) * (SUB * NSUB);
            const int br = bh >> 5, h = bh & 31;
            const size_t off = ((size_t)h * SK + key0) * ROWB;
            const unsigned long long kb = (unsigned long long)(uintptr_t)(br ? a.k[1] : a.k[0]) + off, vb = (unsigned long long)(uintptr_t)(br ? a.v[1] : a.v[0]) + off;
            for (int j = 0; j < NSUB * 8; ++j) {
                const unsigned f = (unsigned)(s * NSUB * 8 + j);
                const unsigned long long src = ((j & 4) ? vb : kb) + (size_t)((j >> 3) * 4 + (j & 3)) * SLOT_BYTES;
                if (f >= NSLOT) {
                    const unsigned need = f - NSLOT + 1;
                    if (lds_ld(lds0 + FREE_OFF + slot * 4) < need) {
                        asm volatile("s_waitcnt vmcnt(0)" ::: "memory");
                        pub = f; lds_st(lds0 + FULL_OFF, pub);
                        for (unsigned sp = 0; lds_ld(lds0 + FREE_OFF + slot * 4) < need && sp < SPIN_CAP; ++sp) __builtin_amdgcn_s_sleep(1);
                    }
                }
                const unsigned dst = lds0 + slot * SLOT_BYTES;
#pragma unroll
                for (int hf = 0; hf < 2; ++hf) {
                    glds3(voff, src + hf * 6144, dst + hf * 6144);
                    glds3(voff, src + hf * 6144 + 3072, dst + hf * 6144 + 3072);
                    asm volatile("s_waitcnt vmcnt(%0)" ::"i"(INFL) : "memory");
                    const int landed = (int)(f * 12 + hf * 6 + 6) - INFL;
                    const unsigned np = landed > 0 ? (unsigned)landed / 12u : 0u;
                    if (np > pub) { pub = np; lds_st(lds0 + FULL_OFF, pub); }
                }
                slot = (slot == NSLOT - 1) ? 0 : slot + 1;
            }
        }
        asm volatile("s_waitcnt vmcnt(0)" ::: "memory");
        lds_st(lds0 + FULL_OFF, (unsigned)NFILL);
    } else {
        const int g = lane >> 4, c = lane & 15;
        const unsigned kaddr = c * ROWB + g * 16;
        const unsigned vaddr = (4 * g + (c >> 2)) * ROWB + (c & 3) * 8;
        auto wait_full = [&](unsigned f) { for (unsigned sp = 0; lds_ld(lds0 + FULL_OFF) <= f && sp < SPIN_CAP; ++sp) __builtin_amdgcn_s_sleep(2); };
        for (int s = wave - 1; s < NSTEP; s += NCONS) {
            const int fb = s * NWG + wg, bh = fb / NP, pi = fb % NP;
            v4u q[6];
#pragma unroll
            for (int i = 0; i < 6; ++i) q[i] = *(const GAS v4u*)(a.ws + WS_Q + (size_t)bh * ROWB + i * 64 + g * 16);
            float m = -INFINITY, l = 0.f;
            f32x4 acc[12];
#pragma unroll
            for (int b = 0; b < 12; ++b) acc[b] = f32x4{0.f, 0.f, 0.f, 0.f};
#pragma unroll 1
            for (int j = 0; j < NSUB; ++j) {
                const unsigned f0 = (unsigned)((s * NSUB + j) * 8);
                f32x4 S[8];
#pragma unroll
                for (int t = 0; t < 4; ++t) {
                    const unsigned f = f0 + t, slot = f % NSLOT;
                    wait_full(f);
                    const LAS unsigned char* kp = (const LAS unsigned char*)(lds + slot * SLOT_BYTES) + kaddr;
                    v4u kf[12];
#pragma unroll
                    for (int i = 0; i < 12; ++i) kf[i] = *(const LAS v4u*)(kp + (i / 6) * 6144 + (i % 6) * 64);
                    asm volatile("s_waitcnt lgkmcnt(0)" ::: "memory");
                    lds_st(lds0 + FREE_OFF + slot * 4, f + 1);
#pragma unroll
                    for (int i = 0; i < 12; ++i) asm volatile("" : "+v"(kf[i]));
#pragma unroll
                    for (int u = 0; u < 2; ++u) { f32x4 x = {0.f, 0.f, 0.f, 0.f};
#pragma unroll
                        for (int i = 0; i < 6; ++i) x = mfma16(kf[u * 6 + i], q[i], x);
                        S[2 * t + u] = x; }
                }
                float mx = S[0][0];
#pragma unroll
                for (int t = 0; t < 8; ++t)
#pragma unroll
                    for (int r = 0; r < 4; ++r) mx = fmaxf(mx, S[t][r]);
                mx = xor32_max(xor16_max(mx));
                const float mn = fmaxf(m, mx * C2);
                const float alpha = __builtin_amdgcn_exp2f(m - mn);
                m = mn;
                float ls = 0.f;
                v4u P[4];
#pragma unroll
                for (int t = 0; t < 4; ++t) {
                    float p[8];
#pragma unroll
                    for (int r = 0; r < 8; ++r) { p[r] = __builtin_amdgcn_exp2f(fmaf(S[2 * t + (r >> 2)][r & 3], C2, -mn)); ls += p[r]; }
                    P[t] = v4u{cvt_pk_bf16(p[0], p[1]), cvt_pk_bf16(p[2], p[3]), cvt_pk_bf16(p[4], p[5]), cvt_pk_bf16(p[6], p[7])};
                }
                l = fmaf(l, alpha, ls);
                if (NSUB > 1) {
#pragma unroll
                    for (int b = 0; b < 12; ++b) acc[b] *= alpha;
                }
#pragma unroll
                for (int t = 0; t < 4; ++t) {
                    const unsigned f = f0 + 4 + t, slot = f % NSLOT;
                    wait_full(f);
                    const LAS unsigned char* vp = (const LAS unsigned char*)(lds + slot * SLOT_BYTES) + vaddr;
                    v2u vf[24];
#pragma unroll
                    for (int i = 0; i < 24; ++i)
                        vf[i] = __builtin_bit_cast(v2u, __builtin_amdgcn_ds_read_tr16_b64_v4i16((LAS s16x4*)(vp + (i & 1) * 6144 + (i >> 1) * 32)));
                    asm volatile("s_waitcnt lgkmcnt(0)" ::: "memory");
                    lds_st(lds0 + FREE_OFF + slot * 4, f + 1);
#pragma unroll
                    for (int i = 0; i < 24; ++i) asm volatile("" : "+v"(vf[i]));
#pragma unroll
                    for (int b = 0; b < 12; ++b) acc[b] = mfma16(v4u{vf[2 * b].x, vf[2 * b].y, vf[2 * b + 1].x, vf[2 * b + 1].y}, P[t], acc[b]);
                }
            }
            l = xor32_sum(xor16_sum(l));
            f32x4 o = acc[0];
#pragma unroll
            for (int b = 1; b < 12; ++b) o = (c == b) ? acc[b] : o;
            const size_t pidx = (size_t)bh * NP + pi;
            if (c < 12) *(GAS f32x4*)(a.ws + WS_PO + pidx * (DH * 4) + c * 64 + g * 16) = o;
            if (lane == 12) *(GAS f32x2*)(a.ws + WS_PML + pidx * 8) = f32x2{m, l};
        }
    }
}

__global__ void __launch_bounds__(256) k_combine(const unsigned short* __restrict__ woa, const unsigned short* __restrict__ wob, unsigned char* __restrict__ ws) {
    __shared__ float wgt[256];
    __shared__ float red[16][48];
    __shared__ float os[48];
    __shared__ float sc[8];
    __shared__ float ored[3][DM];
    const int tid = threadIdx.x, lane = tid & 63, wave = tid >> 6;
    const int bh = blockIdx.x >> 2, js = blockIdx.x & 3, b = bh >> 5, h = bh & 31;
    const unsigned short* wo = (b ? wob : woa) + (size_t)(h * DH + js * 48) * DM;
    const int rg3 = tid / 80, cc = tid % 80;
    v4u w[16];
    if (tid < 240) {
#pragma unroll
        for (int i = 0; i < 16; ++i) w[i] = __builtin_nontemporal_load((const GAS v4u*)(wo + (size_t)(rg3 * 16 + i) * DM + cc * 8));
    }
    const int rg = tid >> 4, c4 = tid & 15;
    constexpr int NR = NP / 16;
    f32x4 po[NR];
    if (c4 < 12) {
#pragma unroll
        for (int i = 0; i < NR; ++i) po[i] = *(const GAS f32x4*)(ws + WS_PO + ((size_t)bh * NP + rg + 16 * i) * (DH * 4) + js * 192 + c4 * 16);
    }
    f32x2 ml = f32x2{-INFINITY, 0.f};
    if (tid < NP) ml = *(const GAS f32x2*)(ws + WS_PML + ((size_t)bh * NP + tid) * 8);
    float M = ml[0];
#pragma unroll
    for (int d = 1; d < 64; d <<= 1) M = fmaxf(M, __shfl_xor(M, d));
    if (lane == 0) sc[wave] = M;
    __syncthreads();
    M = fmaxf(fmaxf(sc[0], sc[1]), fmaxf(sc[2], sc[3]));
    const float wv = (tid < NP) ? __builtin_amdgcn_exp2f(ml[0] - M) : 0.f;
    wgt[tid] = wv;
    float L = wv * ml[1];
#pragma unroll
    for (int d = 1; d < 64; d <<= 1) L += __shfl_xor(L, d);
    if (lane == 0) sc[4 + wave] = L;
    __syncthreads();
    L = (sc[4] + sc[5]) + (sc[6] + sc[7]);
    if (c4 < 12) {
        f32x4 s = {0.f, 0.f, 0.f, 0.f};
#pragma unroll
        for (int i = 0; i < NR; ++i) s += po[i] * wgt[rg + 16 * i];
#pragma unroll
        for (int k = 0; k < 4; ++k) red[rg][c4 * 4 + k] = s[k];
    }
    __syncthreads();
    if (tid < 48) { float s = 0.f;
#pragma unroll
        for (int r = 0; r < 16; ++r) s += red[r][tid];
        os[tid] = (float)(__bf16)(s / L); }
    __syncthreads();
    if (tid < 240) {
        float acc[8] = {0.f, 0.f, 0.f, 0.f, 0.f, 0.f, 0.f, 0.f};
#pragma unroll
        for (int i = 0; i < 16; ++i) { const float ov = os[rg3 * 16 + i];
#pragma unroll
            for (int k = 0; k < 4; ++k) { acc[2 * k] = fmaf(ov, bflo(w[i][k]), acc[2 * k]); acc[2 * k + 1] = fmaf(ov, bfhi(w[i][k]), acc[2 * k + 1]); } }
#pragma unroll
        for (int k = 0; k < 8; ++k) ored[rg3][cc * 8 + k] = acc[k];
    }
    __syncthreads();
    for (int n = tid; n < DM; n += 256) ((float*)(ws + WS_OP))[(size_t)blockIdx.x * DM + n] = (ored[0][n] + ored[1][n]) + ored[2][n];
}

__global__ void __launch_bounds__(256) k_final(const unsigned char* __restrict__ ws, __bf16* __restrict__ out) {
    __shared__ float red[16][64];
    const int b = blockIdx.x / 10, cb = blockIdx.x % 10, tid = threadIdx.x, rg = tid >> 4, c4 = tid & 15;
    f32x4 v[8];
#pragma unroll
    for (int i = 0; i < 8; ++i) v[i] = *(const GAS f32x4*)(ws + WS_OP + ((size_t)(b * 128 + rg + 16 * i) * DM + cb * 64 + c4 * 4) * 4);
    f32x4 s = v[0];
#pragma unroll
    for (int i = 1; i < 8; ++i) s += v[i];
#pragma unroll
    for (int k = 0; k < 4; ++k) red[rg][c4 * 4 + k] = s[k];
    __syncthreads();
    if (tid < 64) { float t = 0.f;
#pragma unroll
        for (int r = 0; r < 16; ++r) t += red[r][tid];
        out[b * DM + cb * 64 + tid] = (__bf16)t; }
}

extern "C" void kernel_launch(void* const* d_in, const int* in_sizes, int n_in, void* d_out, int out_size, void* d_ws, size_t ws_size, hipStream_t stream) {
    (void)in_sizes; (void)n_in; (void)out_size; (void)ws_size;
    const unsigned short* xa = (const unsigned short*)d_in[0];
    const unsigned char* ka = (const unsigned char*)d_in[1];
    const unsigned char* va = (const unsigned char*)d_in[2];
    const unsigned short* wqa = (const unsigned short*)d_in[3];
    const unsigned short* woa = (const unsigned short*)d_in[4];
    const unsigned short* xb = (const unsigned short*)d_in[5];
    const unsigned char* kb = (const unsigned char*)d_in[6];
    const unsigned char* vb = (const unsigned char*)d_in[7];
    const unsigned short* wqb = (const unsigned short*)d_in[8];
    const unsigned short* wob = (const unsigned short*)d_in[9];
    unsigned char* ws = (unsigned char*)d_ws;
    (void)hipFuncSetAttribute((const void*)k_stream, hipFuncAttributeMaxDynamicSharedMemorySize, LDS_BYTES);
    hipLaunchKernelGGL(k_qproj, dim3(192), dim3(256), 0, stream, xa, wqa, xb, wqb, ws);
    StreamArgs sa; sa.k[0] = ka; sa.k[1] = kb; sa.v[0] = va; sa.v[1] = vb; sa.ws = ws;
    hipLaunchKernelGGL(k_stream, dim3(NWG), dim3((1 + NCONS) * 64), LDS_BYTES, stream, sa);
    hipLaunchKernelGGL(k_combine, dim3(256), dim3(256), 0, stream, woa, wob, ws);
    hipLaunchKernelGGL(k_final, dim3(20), dim3(256), 0, stream, (const unsigned char*)ws, (__bf16*)d_out);
}
```

```cpp
#include <hip/hip_runtime.h>
#include <hip/hip_bf16.h>

#define GAS __attribute__((address_space(1)))
#define LAS __attribute__((address_space(3)))
typedef unsigned v4u __attribute__((ext_vector_type(4)));
typedef unsigned v2u __attribute__((ext_vector_type(2)));
typedef float f32x4 __attribute__((ext_vector_type(4)));
typedef float f32x2 __attribute__((ext_vector_type(2)));
typedef __bf16 bf16x8 __attribute__((ext_vector_type(8)));
typedef short s16x4 __attribute__((ext_vector_type(4)));

#define K_INFL 20
#define K_NCONS 2
#define K_NSLOT 10
#define K_NSHARD 8
#define K_TAILH 6

constexpr int DM = 640, NH = 32, DH = 192, SK = 32768, HD = NH * DH;
constexpr int NBH = 2 * NH;
constexpr int NWG = 256;
constexpr int ROWB = DH * 2;
constexpr int SLOT_BYTES = 32 * ROWB;
constexpr int NSLOT = K_NSLOT;
constexpr int INFL = K_INFL;
constexpr int NCONS = K_NCONS;
constexpr int NWAVE = 2 + NCONS;
constexpr int TAILH = K_TAILH, NSH = NBH - TAILH;
constexpr int NJA = NH / 2, NJB = (NSH - NH) / 2, NJ = NJA + NJB;
static_assert(NSH > NH && (NSH & 1) == 0, "schedule");
constexpr int NSTATIC = 2 * NJ;
constexpr int NTAIL = TAILH * 256;
constexpr int NSHARD = K_NSHARD;
constexpr int CTR_STRIDE = 4096 + 256;
constexpr int NREP = 8;
constexpr unsigned SENT = 0xFFFFFFFFu;
constexpr int PREC = 400;
constexpr int NPC = 25;
constexpr int RING_BYTES = NSLOT * SLOT_BYTES;
constexpr int FULL_OFF = RING_BYTES, TKCNT_OFF = RING_BYTES + 4, LSTART_OFF = RING_BYTES + 8, NDONE_OFF = RING_BYTES + 12, SCNT_OFF = RING_BYTES + 16, FREE_OFF = RING_BYTES + 64, TK_OFF = RING_BYTES + 128;
constexpr int P_OFF = RING_BYTES + 256;
constexpr int PART_OFF = P_OFF + NJA * 512;
constexpr int CAPT = (160 * 1024 - PART_OFF) / PREC - NJ;
constexpr int LDS_BYTES = PART_OFF + (NJ + CAPT) * PREC;
static_assert((NSHARD + NREP) * CTR_STRIDE <= 131072, "counters");
static_assert(LDS_BYTES <= 160 * 1024 && NSLOT <= 16 && NSLOT >= 9 && NCONS <= 4 && CAPT * (NWG / NSHARD) * NSHARD >= NTAIL + NWG, "LDS map");
constexpr float C2 = 0.07216878364870322f * 1.4426950408889634f;
constexpr unsigned SPIN_CAP = 1u << 22;

constexpr size_t WS_CTR = 0;
constexpr size_t WS_Q = 131072;
constexpr size_t WS_PML = 196608;
constexpr size_t WS_PO = WS_PML + (size_t)NBH * 256 * 8;
constexpr size_t WS_OP = WS_PO + (size_t)NBH * 256 * DH * 2;
constexpr size_t WS_END = WS_OP + (size_t)256 * DM * 4;

__device__ __forceinline__ float bflo(unsigned u) { return __builtin_bit_cast(float, u << 16); }
__device__ __forceinline__ float bfhi(unsigned u) { return __builtin_bit_cast(float, u & 0xffff0000u); }
__device__ __forceinline__ unsigned cvt_pk_bf16(float lo, float hi) {
    typedef __bf16 b2 __attribute__((ext_vector_type(2)));
    b2 r = {(__bf16)lo, (__bf16)hi};
    return __builtin_bit_cast(unsigned, r);
}
__device__ __forceinline__ f32x4 mfma16(v4u a, v4u b, f32x4 c) {
    return __builtin_amdgcn_mfma_f32_16x16x32_bf16(__builtin_bit_cast(bf16x8, a), __builtin_bit_cast(bf16x8, b), c, 0, 0, 0);
}
__device__ __forceinline__ float xor16_max(float x) { auto s = __builtin_amdgcn_permlane16_swap(__float_as_uint(x), __float_as_uint(x), false, false); return fmaxf(__uint_as_float(s[0]), __uint_as_float(s[1])); }
__device__ __forceinline__ float xor32_max(float x) { auto s = __builtin_amdgcn_permlane32_swap(__float_as_uint(x), __float_as_uint(x), false, false); return fmaxf(__uint_as_float(s[0]), __uint_as_float(s[1])); }
__device__ __forceinline__ float xor16_sum(float x) { auto s = __builtin_amdgcn_permlane16_swap(__float_as_uint(x), __float_as_uint(x), false, false); return __uint_as_float(s[0]) + __uint_as_float(s[1]); }
__device__ __forceinline__ float xor32_sum(float x) { auto s = __builtin_amdgcn_permlane32_swap(__float_as_uint(x), __float_as_uint(x), false, false); return __uint_as_float(s[0]) + __uint_as_float(s[1]); }

__device__ __forceinline__ void lds_st(unsigned addr, unsigned v) { asm volatile("ds_write_b32 %0, %1" : : "v"(addr), "v"(v) : "memory"); }
__device__ __forceinline__ unsigned lds_ld(unsigned addr) { unsigned v; asm volatile("ds_read_b32 %0, %1\n\ts_waitcnt lgkmcnt(0)" : "=&v"(v) : "v"(addr) : "memory"); return __builtin_amdgcn_readfirstlane(v); }
__device__ __forceinline__ void glds3(unsigned voff, unsigned long long sbase, unsigned ldsdst) {
    unsigned keep;
    asm volatile("s_mov_b32 %0, m0\n\ts_mov_b32 m0, %3\n\ts_nop 0\n\t"
                 "global_load_lds_dwordx4 %1, %2 nt\n\t"
                 "global_load_lds_dwordx4 %1, %2 offset:1024 nt\n\t"
                 "global_load_lds_dwordx4 %1, %2 offset:2048 nt\n\t"
                 "s_mov_b32 m0, %0"
                 : "=&s"(keep) : "v"(voff), "s"(sbase), "s"(ldsdst) : "memory");
}

static_assert(192 * 256 >= NWG * DM / 4, "k_qproj fills the output partials");
__global__ void __launch_bounds__(256) k_qproj(const unsigned short* __restrict__ xa, const unsigned short* __restrict__ wqa,
                                               const unsigned short* __restrict__ xb, const unsigned short* __restrict__ wqb, unsigned char* __restrict__ ws) {
    __shared__ float red[32][64 + 1];
    __shared__ float xs[DM];
    const int b = blockIdx.x / 96, cb = blockIdx.x % 96, tid = threadIdx.x;
    const unsigned short* x = b ? xb : xa; const unsigned short* wq = b ? wqb : wqa;
    const int rg = tid >> 3, c8 = tid & 7;
    if (blockIdx.x == 0 && tid < NSHARD + NREP) *(unsigned*)(ws + WS_CTR + tid * CTR_STRIDE) = 0u;
    {   const v4u sent = {SENT, SENT, SENT, SENT};
        const int i = blockIdx.x * 256 + tid;
        if (i < NWG * DM / 4) *(v4u*)(ws + WS_OP + (size_t)i * 16) = sent;
        if (i < TAILH * 256 / 2) *(v4u*)(ws + WS_PML + (size_t)NSH * 256 * 8 + (size_t)i * 16) = sent;
        for (int k = i; k < TAILH * 256 * DH / 8; k += 192 * 256) *(v4u*)(ws + WS_PO + (size_t)NSH * 256 * DH * 2 + (size_t)k * 16) = sent; }
    v4u w[20];
#pragma unroll
    for (int i = 0; i < 20; ++i) w[i] = __builtin_nontemporal_load((const GAS v4u*)(wq + (size_t)(rg + 32 * i) * HD + cb * 64 + c8 * 8));
    for (int i = tid; i < DM; i += 256) xs[i] = bflo(x[i]);
    __syncthreads();
    float a[8] = {0.f, 0.f, 0.f, 0.f, 0.f, 0.f, 0.f, 0.f};
#pragma unroll
    for (int i = 0; i < 20; ++i) { const float xv = xs[rg + 32 * i];
#pragma unroll
        for (int k = 0; k < 4; ++k) { a[2 * k] = fmaf(xv, bflo(w[i][k]), a[2 * k]); a[2 * k + 1] = fmaf(xv, bfhi(w[i][k]), a[2 * k + 1]); } }
#pragma unroll
    for (int k = 0; k < 8; ++k) red[rg][c8 * 8 + k] = a[k];
    __syncthreads();
    if (tid < 64) { float s = 0.f;
#pragma unroll
        for (int r = 0; r < 32; ++r) s += red[r][tid];
        ((__bf16*)(ws + WS_Q))[b * HD + cb * 64 + tid] = (__bf16)s; }
}

struct StreamArgs { const unsigned char* k[2]; const unsigned char* v[2]; const unsigned short* wo[2]; unsigned char* ws; __bf16* out; };

__global__ void __launch_bounds__(NWAVE * 64, 1) k_stream(StreamArgs a) {
    extern __shared__ __attribute__((aligned(16))) unsigned char lds[];
    const int tid = threadIdx.x, lane = tid & 63, wave = __builtin_amdgcn_readfirstlane(tid >> 6);
    const int wg = blockIdx.x;
    const unsigned lds0 = (unsigned)(uintptr_t)lds;
    if (tid < 64) lds_st(lds0 + FULL_OFF + tid * 4, 0u);
    const int rg3 = tid / 80, cc = tid % 80;
    v4u wo_r[16];
    {   const unsigned short* wo = ((wg >> 7) ? a.wo[1] : a.wo[0]) + (size_t)(((wg >> 2) & 31) * DH + (wg & 3) * 48) * DM;
#pragma unroll
        for (int i = 0; i < 16; ++i) wo_r[i] = __builtin_nontemporal_load((const GAS v4u*)(wo + (size_t)((rg3 < 3 ? rg3 : 2) * 16 + i) * DM + cc * 8)); }
    asm volatile("s_waitcnt lgkmcnt(0)\n\ts_barrier" ::: "memory");
    auto wait_gt = [&](unsigned addr, unsigned x, unsigned& seen) {
        if (seen > x) return;
        for (unsigned sp = 0; (seen = lds_ld(addr)) <= x && sp < SPIN_CAP; ++sp) __builtin_amdgcn_s_sleep(1);
    };
    const __amdgpu_buffer_rsrc_t rs = __builtin_amdgcn_make_buffer_rsrc((void*)a.ws, 0, (int)WS_END, 0x00020000);
    constexpr int SC1 = 16;
    GAS unsigned char* arr = (GAS unsigned char*)(a.ws + WS_CTR + NSHARD * CTR_STRIDE);
    auto put_record = [&](int r, int e) {
        const unsigned pidx = __float_as_uint(*(const LAS float*)(lds + PART_OFF + r * PREC + DH * 2 + 8));
        v4u v = *(const LAS v4u*)(lds + PART_OFF + r * PREC + e * 16);
#pragma unroll
        for (int k = 0; k < 4; ++k) v[k] = v[k] == SENT ? 0x7FC07FC0u : v[k];
        if (e < NPC - 1) __builtin_amdgcn_raw_buffer_store_b128(v, rs, (int)WS_PO + pidx * (DH * 2) + e * 16, 0, SC1);
        else __builtin_amdgcn_raw_buffer_store_b64(v2u{v[0], v[1]}, rs, (int)WS_PML + pidx * 8, 0, SC1);
    };
    const int skey = (wg & 127) * 256;

    if (wave == 0) {
        const unsigned voff = lane * 16;
        unsigned pub = 0, slot = 0, f = 0, tkseen = 0;
        auto fill = [&](unsigned long long src) {
            if (f >= NSLOT) {
                const unsigned need = f - NSLOT + 1;
                if (lds_ld(lds0 + FREE_OFF + slot * 4) < need) {
                    asm volatile("s_waitcnt vmcnt(0)" ::: "memory");
                    pub = f; lds_st(lds0 + FULL_OFF, pub);
                    for (unsigned sp = 0; lds_ld(lds0 + FREE_OFF + slot * 4) < need && sp < SPIN_CAP; ++sp) __builtin_amdgcn_s_sleep(1);
                }
            }
            const unsigned dst = lds0 + slot * SLOT_BYTES;
#pragma unroll
            for (int hf = 0; hf < 2; ++hf) {
                glds3(voff, src + hf * 6144, dst + hf * 6144);
                glds3(voff, src + hf * 6144 + 3072, dst + hf * 6144 + 3072);
                asm volatile("s_waitcnt vmcnt(%0)" ::"i"(INFL) : "memory");
                const int landed = (int)(f * 12 + hf * 6 + 6) - INFL;
                const unsigned np = landed > 0 ? (unsigned)landed / 12u : 0u;
                if (np > pub) { pub = np; lds_st(lds0 + FULL_OFF, pub); }
            }
            slot = (slot == NSLOT - 1) ? 0 : slot + 1;
            ++f;
        };
        for (int i = 0; i < NSTATIC; ++i) {
            const int br = i >= 2 * NJA, r = br ? i - 2 * NJA : i, nj = br ? NJB : NJA, kv = r >= nj;
            const int bh = 2 * ((br ? NJA : 0) + (kv ? r - nj : r)) + (wg >> 7);
            const unsigned char* base = kv ? (br ? a.v[1] : a.v[0]) : (br ? a.k[1] : a.k[0]);
            const unsigned long long src = (unsigned long long)(uintptr_t)base + ((size_t)(bh & 31) * SK + skey) * ROWB;
            if (i == NSTATIC - 1) lds_st(lds0 + LSTART_OFF, 1u);
            for (int t = 0; t < 8; ++t) fill(src + t * SLOT_BYTES);
        }
        for (unsigned n = 0;; ++n) {
            wait_gt(lds0 + TKCNT_OFF, n, tkseen);
            const unsigned t = lds_ld(lds0 + TK_OFF + (n & 7) * 4);
            if (t >= (unsigned)NTAIL) break;
            lds_st(lds0 + LSTART_OFF, n + 2);
            const int bh = NSH + (t >> 8);
            const size_t off = ((size_t)(bh & 31) * SK + (t & 255) * 128) * ROWB;
            const unsigned long long kb = (unsigned long long)(uintptr_t)a.k[1] + off, vb = (unsigned long long)(uintptr_t)a.v[1] + off;
            for (int j = 0; j < 8; ++j) fill(((j & 4) ? vb : kb) + (size_t)(j & 3) * SLOT_BYTES);
        }
        asm volatile("s_waitcnt vmcnt(0)" ::: "memory");
        lds_st(lds0 + FULL_OFF, f);
    } else if (wave == 1) {
        const int shard = (wg >> 3) % NSHARD;
        GAS unsigned* ctr = (GAS unsigned*)(a.ws + WS_CTR + shard * CTR_STRIDE);
        unsigned n = 0, started = 0;
        int pubstate = 0;
        auto publish_static = [&]() {
            for (int i = lane; i < NJ * NPC; i += 64) put_record(i / NPC, i % NPC);
            pubstate = 1;
        };
        auto signal_static = [&]() {
            asm volatile("s_waitcnt vmcnt(0)" ::: "memory");
            if (lane < NREP) __hip_atomic_fetch_add((GAS unsigned*)(arr + lane * CTR_STRIDE), 1u, __ATOMIC_RELAXED, __HIP_MEMORY_SCOPE_AGENT);
            pubstate = 2;
        };
        for (; n < (unsigned)CAPT; ++n) {
            for (unsigned sp = 0; started <= n && (started = lds_ld(lds0 + LSTART_OFF)) <= n && sp < (SPIN_CAP << 4); ++sp) {
                if (pubstate == 0 && lds_ld(lds0 + SCNT_OFF) == (unsigned)NCONS) publish_static();
                __builtin_amdgcn_s_sleep(8);
            }
            unsigned j = 0;
            if (lane == 0) j = __hip_atomic_fetch_add(ctr, 1u, __ATOMIC_RELAXED, __HIP_MEMORY_SCOPE_AGENT);
            j = __builtin_amdgcn_readfirstlane(j);
            const unsigned t = j * NSHARD + shard;
            if (t < (unsigned)NTAIL) {
                lds_st(lds0 + TK_OFF + (n & 7) * 4, t);
                asm volatile("s_waitcnt lgkmcnt(0)" ::: "memory");
                lds_st(lds0 + TKCNT_OFF, n + 1);
            }
            if (pubstate == 1) signal_static();
            if (t >= (unsigned)NTAIL) break;
        }
        if (n == (unsigned)CAPT) wait_gt(lds0 + LSTART_OFF, n, started);
        lds_st(lds0 + NDONE_OFF, n);
#pragma unroll
        for (int i = 0; i < NCONS; ++i) lds_st(lds0 + TK_OFF + ((n + i) & 7) * 4, (unsigned)NTAIL);
        asm volatile("s_waitcnt lgkmcnt(0)" ::: "memory");
        lds_st(lds0 + TKCNT_OFF, n + NCONS);
        if (pubstate == 0) {
            for (unsigned sp = 0; lds_ld(lds0 + SCNT_OFF) != (unsigned)NCONS && sp < (SPIN_CAP << 4); ++sp) __builtin_amdgcn_s_sleep(2);
            publish_static();
        }
        if (pubstate == 1) signal_static();
    } else {
        const int cw = wave - 2;
        const int g = lane >> 4, c = lane & 15;
        const unsigned kaddr = c * ROWB + g * 16;
        const unsigned vaddr = (4 * g + (c >> 2)) * ROWB + (c & 3) * 8;
        unsigned fullseen = 0, tkseen = 0;
        auto load_q = [&](v4u (&q)[6], int bh) {
#pragma unroll
            for (int i = 0; i < 6; ++i) q[i] = *(const GAS v4u*)(a.ws + WS_Q + (size_t)bh * ROWB + i * 64 + g * 16);
        };
        auto qk_slot = [&](unsigned f, const v4u (&q)[6], f32x4& x0, f32x4& x1) {
            const unsigned slot = f % NSLOT;
            wait_gt(lds0 + FULL_OFF, f, fullseen);
            const LAS unsigned char* kp = (const LAS unsigned char*)(lds + slot * SLOT_BYTES) + kaddr;
            v4u kf[12];
#pragma unroll
            for (int i = 0; i < 12; ++i) kf[i] = *(const LAS v4u*)(kp + (i / 6) * 6144 + (i % 6) * 64);
            asm volatile("s_waitcnt lgkmcnt(0)" ::: "memory");
            lds_st(lds0 + FREE_OFF + slot * 4, f + 1);
#pragma unroll
            for (int i = 0; i < 12; ++i) asm volatile("" : "+v"(kf[i]));
            x0 = f32x4{0.f, 0.f, 0.f, 0.f}; x1 = x0;
#pragma unroll
            for (int i = 0; i < 6; ++i) { x0 = mfma16(kf[i], q[i], x0); x1 = mfma16(kf[6 + i], q[i], x1); }
        };
        auto pv_slot = [&](unsigned f, v4u P, f32x4 (&acc)[12]) {
            const unsigned slot = f % NSLOT;
            wait_gt(lds0 + FULL_OFF, f, fullseen);
            const LAS unsigned char* vp = (const LAS unsigned char*)(lds + slot * SLOT_BYTES) + vaddr;
            v2u vf[24];
#pragma unroll
            for (int i = 0; i < 24; ++i)
                vf[i] = __builtin_bit_cast(v2u, __builtin_amdgcn_ds_read_tr16_b64_v4i16((LAS s16x4*)(vp + (i & 1) * 6144 + (i >> 1) * 32)));
            asm volatile("s_waitcnt lgkmcnt(0)" ::: "memory");
            lds_st(lds0 + FREE_OFF + slot * 4, f + 1);
#pragma unroll
            for (int i = 0; i < 24; ++i) asm volatile("" : "+v"(vf[i]));
#pragma unroll
            for (int b = 0; b < 12; ++b) acc[b] = mfma16(v4u{vf[2 * b].x, vf[2 * b].y, vf[2 * b + 1].x, vf[2 * b + 1].y}, P, acc[b]);
        };
        auto softmax = [&](auto& S, auto& P, float& m, float& l) {
            constexpr int NS = sizeof(P) / sizeof(v4u);
            float mx = S[0][0];
#pragma unroll
            for (int tt = 0; tt < 2 * NS; ++tt)
#pragma unroll
                for (int r = 0; r < 4; ++r) mx = fmaxf(mx, S[tt][r]);
            m = xor32_max(xor16_max(mx)) * C2;
            float ls = 0.f;
#pragma unroll
            for (int tt = 0; tt < NS; ++tt) {
                float p[8];
#pragma unroll
                for (int r = 0; r < 8; ++r) { p[r] = __builtin_amdgcn_exp2f(fmaf(S[2 * tt + (r >> 2)][r & 3], C2, -m)); ls += p[r]; }
                P[tt] = v4u{cvt_pk_bf16(p[0], p[1]), cvt_pk_bf16(p[2], p[3]), cvt_pk_bf16(p[4], p[5]), cvt_pk_bf16(p[6], p[7])};
            }
            l = xor32_sum(xor16_sum(ls));
        };
        auto store_o = [&](int r, const f32x4 (&acc)[12]) {
            if (c == 0) {
#pragma unroll
                for (int b = 0; b < 12; ++b) *(LAS v2u*)((LAS unsigned char*)(lds + PART_OFF + r * PREC) + b * 32 + g * 8) = v2u{cvt_pk_bf16(acc[b][0], acc[b][1]), cvt_pk_bf16(acc[b][2], acc[b][3])};
            }
        };
        auto store_ml = [&](int r, float m, float l, unsigned pidx) {
            if (lane == 0) *(LAS f32x4*)((LAS unsigned char*)(lds + PART_OFF + r * PREC) + DH * 2) = f32x4{m, l, __uint_as_float(pidx), 0.f};
        };
#pragma unroll 1
        for (int br = 0; br < 2; ++br) {
            const int j0 = br ? NJA : 0, nj = br ? NJB : NJA, i0 = br ? 2 * NJA : 0;
            const int jf = j0 + ((cw - j0 % NCONS + NCONS) % NCONS);
            v4u qn[6];
            load_q(qn, 2 * (jf < j0 + nj ? jf : j0) + (wg >> 7));
#pragma unroll 1
            for (int j = jf; j < j0 + nj; j += NCONS) {
                v4u q[6];
#pragma unroll
                for (int i = 0; i < 6; ++i) q[i] = qn[i];
                load_q(qn, 2 * (j + NCONS < j0 + nj ? j + NCONS : j) + (wg >> 7));
                const unsigned f0 = (unsigned)(i0 + j - j0) * 8;
                f32x4 S[16];
#pragma unroll
                for (int tt = 0; tt < 8; ++tt) qk_slot(f0 + tt, q, S[2 * tt], S[2 * tt + 1]);
                v4u P[8]; float m, l;
                softmax(S, P, m, l);
                if (c == 0) {
#pragma unroll
                    for (int tt = 0; tt < 8; ++tt) *(LAS v4u*)((LAS unsigned char*)(lds + P_OFF + (j - j0) * 512) + tt * 64 + g * 16) = P[tt];
                }
                store_ml(j, m, l, (unsigned)((2 * j + (wg >> 7)) * 256 + (wg & 127)));
            }
#pragma unroll 1
            for (int j = jf; j < j0 + nj; j += NCONS) {
                const unsigned f0 = (unsigned)(i0 + nj + j - j0) * 8;
                f32x4 acc[12];
#pragma unroll
                for (int b = 0; b < 12; ++b) acc[b] = f32x4{0.f, 0.f, 0.f, 0.f};
#pragma unroll
                for (int tt = 0; tt < 8; ++tt) {
                    const v4u P = *(const LAS v4u*)((const LAS unsigned char*)(lds + P_OFF + (j - j0) * 512) + tt * 64 + g * 16);
                    pv_slot(f0 + tt, P, acc);
                }
                store_o(j, acc);
            }
        }
        asm volatile("s_waitcnt lgkmcnt(0)" ::: "memory");
        if (lane == 0) __hip_atomic_fetch_add((LAS unsigned*)(lds + SCNT_OFF), 1u, __ATOMIC_RELAXED, __HIP_MEMORY_SCOPE_WORKGROUP);
#pragma unroll 1
        for (unsigned n = cw;; n += NCONS) {
            wait_gt(lds0 + TKCNT_OFF, n, tkseen);
            const unsigned t = lds_ld(lds0 + TK_OFF + (n & 7) * 4);
            if (t >= (unsigned)NTAIL) break;
            v4u q[6];
            load_q(q, NSH + (t >> 8));
            const unsigned f0 = (NSTATIC + n) * 8;
            f32x4 S[8];
#pragma unroll
            for (int tt = 0; tt < 4; ++tt) qk_slot(f0 + tt, q, S[2 * tt], S[2 * tt + 1]);
            v4u P[4]; float m, l;
            softmax(S, P, m, l);
            f32x4 acc[12];
#pragma unroll
            for (int b = 0; b < 12; ++b) acc[b] = f32x4{0.f, 0.f, 0.f, 0.f};
#pragma unroll
            for (int tt = 0; tt < 4; ++tt) pv_slot(f0 + 4 + tt, P[tt], acc);
            store_o(NJ + n, acc);
            store_ml(NJ + n, m, l, NSH * 256 + t);
        }
    }
    asm volatile("s_waitcnt vmcnt(0) lgkmcnt(0)\n\ts_barrier" ::: "memory");
    const int ntl = (int)*(const LAS unsigned*)(lds + NDONE_OFF);
    for (int i = tid; i < ntl * NPC; i += NWAVE * 64) put_record(NJ + i / NPC, i % NPC);
    if ((wg >> 2) < NSH) {
        if (tid == 0) {
            GAS unsigned* mine = (GAS unsigned*)(arr + (wg % NREP) * CTR_STRIDE);
            for (unsigned sp = 0; __hip_atomic_load(mine, __ATOMIC_RELAXED, __HIP_MEMORY_SCOPE_AGENT) < (unsigned)NWG && sp < SPIN_CAP; ++sp) __builtin_amdgcn_s_sleep(1);
            __builtin_amdgcn_fence(__ATOMIC_ACQUIRE, "wavefront");
        }
        asm volatile("s_waitcnt vmcnt(0) lgkmcnt(0)\n\ts_barrier" ::: "memory");
    }
    {
        LAS float* red = (LAS float*)lds;
        LAS float* os = red + 32 * 52;
        LAS float* ored = os + 48;
        const int bh = wg >> 2, js = wg & 3;
        const int rg = (tid >> 3) & 31, c8 = tid & 7;
        const int prow = (int)WS_PO + (bh * 256 + rg) * (DH * 2) + js * 96 + (c8 < 6 ? c8 : 0) * 16, pml = (int)WS_PML + (bh * 256 + rg) * 8;
        v4u po[8]; v2u ml[8];
        auto has_data = [&](int i) { return po[i][0] != SENT && po[i][1] != SENT && po[i][2] != SENT && po[i][3] != SENT && ml[i][0] != SENT && ml[i][1] != SENT; };
        int nr = 4;
        if (bh < NSH) {
#pragma unroll
            for (int i = 0; i < 4; ++i) { po[i] = __builtin_amdgcn_raw_buffer_load_b128(rs, prow + 32 * i * (DH * 2), 0, SC1); ml[i] = __builtin_amdgcn_raw_buffer_load_b64(rs, pml + 32 * i * 8, 0, SC1); }
#pragma unroll
            for (int i = 4; i < 8; ++i) { po[i] = v4u{0u, 0u, 0u, 0u}; ml[i] = v2u{0xFF800000u, 0u}; }
        } else {
            nr = 8;
            for (unsigned sp = 0; sp < SPIN_CAP; ++sp) {
#pragma unroll
                for (int i = 0; i < 8; ++i) { po[i] = __builtin_amdgcn_raw_buffer_load_b128(rs, prow + 32 * i * (DH * 2), 0, SC1); ml[i] = __builtin_amdgcn_raw_buffer_load_b64(rs, pml + 32 * i * 8, 0, SC1); }
                bool ok = true;
#pragma unroll
                for (int i = 0; i < 8; ++i) ok = ok && has_data(i);
                if (__all(ok)) break;
                __builtin_amdgcn_s_sleep(1);
            }
        }
        (void)nr;
        float Mt = __uint_as_float(ml[0][0]);
#pragma unroll
        for (int i = 1; i < 8; ++i) Mt = fmaxf(Mt, __uint_as_float(ml[i][0]));
        float Lt = 0.f, t[8] = {0.f, 0.f, 0.f, 0.f, 0.f, 0.f, 0.f, 0.f};
#pragma unroll
        for (int i = 0; i < 8; ++i) {
            const float w_ = __builtin_amdgcn_exp2f(__uint_as_float(ml[i][0]) - Mt);
            Lt = fmaf(w_, __uint_as_float(ml[i][1]), Lt);
#pragma unroll
            for (int k = 0; k < 4; ++k) { t[2 * k] = fmaf(w_, bflo(po[i][k]), t[2 * k]); t[2 * k + 1] = fmaf(w_, bfhi(po[i][k]), t[2 * k + 1]); }
        }
        if (tid < 256) {
            if (c8 < 6) { *(LAS f32x4*)(red + rg * 52 + c8 * 8) = f32x4{t[0], t[1], t[2], t[3]}; *(LAS f32x4*)(red + rg * 52 + c8 * 8 + 4) = f32x4{t[4], t[5], t[6], t[7]}; }
            else if (c8 == 6) *(LAS f32x2*)(red + rg * 52 + 48) = f32x2{Mt, Lt};
        }
        __syncthreads();
        if (tid < 48) {
            float M = red[48];
#pragma unroll
            for (int r = 1; r < 32; ++r) M = fmaxf(M, red[r * 52 + 48]);
            float L = 0.f, o = 0.f;
#pragma unroll
            for (int r = 0; r < 32; ++r) { const float f = __builtin_amdgcn_exp2f(red[r * 52 + 48] - M); L = fmaf(f, red[r * 52 + 49], L); o = fmaf(f, red[r * 52 + tid], o); }
            os[tid] = (float)(__bf16)(o / L);
        }
        __syncthreads();
        if (tid < 240) {
            float acc[8] = {0.f, 0.f, 0.f, 0.f, 0.f, 0.f, 0.f, 0.f};
#pragma unroll
            for (int i = 0; i < 16; ++i) { const float ov = os[rg3 * 16 + i];
#pragma unroll
                for (int k = 0; k < 4; ++k) { acc[2 * k] = fmaf(ov, bflo(wo_r[i][k]), acc[2 * k]); acc[2 * k + 1] = fmaf(ov, bfhi(wo_r[i][k]), acc[2 * k + 1]); } }
#pragma unroll
            for (int k = 0; k < 8; ++k) ored[rg3 * DM + cc * 8 + k] = acc[k];
        }
        __syncthreads();
        if (tid < DM / 4) {
            v4u v;
#pragma unroll
            for (int k = 0; k < 4; ++k) { const unsigned u = __float_as_uint((ored[tid * 4 + k] + ored[DM + tid * 4 + k]) + ored[2 * DM + tid * 4 + k]); v[k] = u == SENT ? 0x7FC00000u : u; }
            __builtin_amdgcn_raw_buffer_store_b128(v, rs, (int)WS_OP + wg * (DM * 4) + tid * 16, 0, SC1);
        }
    }
    if (wg < 40) {
        LAS float* red2 = (LAS float*)(lds + 16384);
        const int b = wg / 20, cb = wg % 20, rg = (tid >> 3) & 31, c4 = tid & 7;
        v4u v[4];
        for (unsigned sp = 0; sp < SPIN_CAP; ++sp) {
            bool ok = true;
#pragma unroll
            for (int i = 0; i < 4; ++i) { v[i] = __builtin_amdgcn_raw_buffer_load_b128(rs, (int)WS_OP + (b * 128 + rg + 32 * i) * (DM * 4) + cb * 128 + c4 * 16, 0, SC1);
                ok = ok && v[i][0] != SENT && v[i][1] != SENT && v[i][2] != SENT && v[i][3] != SENT; }
            if (__all(ok)) break;
            __builtin_amdgcn_s_sleep(2);
        }
        const f32x4 t = (__builtin_bit_cast(f32x4, v[0]) + __builtin_bit_cast(f32x4, v[1])) + (__builtin_bit_cast(f32x4, v[2]) + __builtin_bit_cast(f32x4, v[3]));
        if (tid < 256) {
#pragma unroll
            for (int k = 0; k < 4; ++k) red2[rg * 32 + c4 * 4 + k] = t[k];
        }
        __syncthreads();
        if (tid < 32) { float u = 0.f;
#pragma unroll
            for (int r = 0; r < 32; ++r) u += red2[r * 32 + tid];
            a.out[b * DM + cb * 32 + tid] = (__bf16)u; }
    }
}

extern "C" void kernel_launch(void* const* d_in, const int* in_sizes, int n_in, void* d_out, int out_size, void* d_ws, size_t ws_size, hipStream_t stream) {
    (void)in_sizes; (void)n_in; (void)out_size; (void)ws_size;
    unsigned char* ws = (unsigned char*)d_ws;
    (void)hipFuncSetAttribute((const void*)k_stream, hipFuncAttributeMaxDynamicSharedMemorySize, LDS_BYTES);
    hipLaunchKernelGGL(k_qproj, dim3(192), dim3(256), 0, stream, (const unsigned short*)d_in[0], (const unsigned short*)d_in[3], (const unsigned short*)d_in[5], (const unsigned short*)d_in[8], ws);
    StreamArgs sa;
    sa.k[0] = (const unsigned char*)d_in[1]; sa.k[1] = (const unsigned char*)d_in[6];
    sa.v[0] = (const unsigned char*)d_in[2]; sa.v[1] = (const unsigned char*)d_in[7];
    sa.wo[0] = (const unsigned short*)d_in[4]; sa.wo[1] = (const unsigned short*)d_in[9];
    sa.ws = ws; sa.out = (__bf16*)d_out;
    hipLaunchKernelGGL(k_stream, dim3(NWG), dim3(NWAVE * 64), LDS_BYTES, stream, sa);
}
```

```cpp
#include <hip/hip_runtime.h>
#include <hip/hip_bf16.h>

#define GAS __attribute__((address_space(1)))
#define LAS __attribute__((address_space(3)))
typedef unsigned v4u __attribute__((ext_vector_type(4)));
typedef unsigned v2u __attribute__((ext_vector_type(2)));
typedef float f32x4 __attribute__((ext_vector_type(4)));
typedef float f32x2 __attribute__((ext_vector_type(2)));
typedef __bf16 bf16x8 __attribute__((ext_vector_type(8)));
typedef short s16x4 __attribute__((ext_vector_type(4)));

#define K_INFL 20
#define K_NCONS 2
#define K_NSLOT 11
#define K_NSHARD 8
#define K_TKEYS 128
#define K_DEPTH 1
#define K_TAILH 6

constexpr int DM = 640, NH = 32, DH = 192, SK = 32768, HD = NH * DH;
constexpr int NBH = 2 * NH;
constexpr int NWG = 256;
constexpr int ROWB = DH * 2;
constexpr int SLOT_BYTES = 32 * ROWB;
constexpr int NSLOT = K_NSLOT;
constexpr int INFL = K_INFL;
constexpr int NCONS = K_NCONS;
constexpr int NWAVE = 2 + NCONS;
constexpr int TAILH = K_TAILH, NSH = NBH - TAILH;
constexpr int NJA = NH / 2, NJB = (NSH - NH) / 2, NJ = NJA + NJB;
static_assert(NSH > NH && (NSH & 1) == 0, "schedule");
constexpr int NSTATIC = 2 * NJ;
constexpr int TKEYS = K_TKEYS, TSL = TKEYS / 32;
constexpr int NTB = SK / TKEYS;
constexpr int NTAIL = TAILH * NTB;
constexpr int DEPTH = K_DEPTH;
static_assert(TKEYS == 64 || TKEYS == 128, "tail block");
constexpr int NSHARD = K_NSHARD;
constexpr int CTR_STRIDE = 4096 + 256;
constexpr int NREP = 8;
constexpr unsigned SENT = 0xFFFFFFFFu;
constexpr int PREC = 400;
constexpr int NPC = 25;
constexpr int RING_BYTES = NSLOT * SLOT_BYTES;
constexpr int FULL_OFF = RING_BYTES, TKCNT_OFF = RING_BYTES + 4, LSTART_OFF = RING_BYTES + 8, NDONE_OFF = RING_BYTES + 12, SCNT_OFF = RING_BYTES + 16, FREE_OFF = RING_BYTES + 64, TK_OFF = RING_BYTES + 128;
constexpr int P_OFF = RING_BYTES + 256;
constexpr int PART_OFF = P_OFF + NJA * 512;
constexpr int CAPT = 64;
constexpr int LDS_BYTES = PART_OFF + (NJ + NCONS) * PREC;
static_assert((NSHARD + NREP) * CTR_STRIDE <= 131072, "counters");
static_assert(LDS_BYTES <= 160 * 1024 && NSLOT <= 16 && NSLOT >= 9 && NCONS <= 4 && CAPT * NWG >= NTAIL + NWG, "LDS map");
constexpr float C2 = 0.07216878364870322f * 1.4426950408889634f;
constexpr unsigned SPIN_CAP = 1u << 22;

constexpr size_t WS_CTR = 0;
constexpr size_t WS_Q = 131072;
constexpr size_t WS_PML = 196608;
constexpr int NROW = NSH * 128 + NTAIL;
constexpr size_t WS_PO = WS_PML + (size_t)NROW * 8;
constexpr size_t WS_OP = WS_PO + (size_t)NROW * DH * 2;
static_assert(WS_PO % 128 == 0, "rows start on cache lines");
constexpr size_t WS_END = WS_OP + (size_t)256 * DM * 4;

__device__ __forceinline__ float bflo(unsigned u) { return __builtin_bit_cast(float, u << 16); }
__device__ __forceinline__ float bfhi(unsigned u) { return __builtin_bit_cast(float, u & 0xffff0000u); }
__device__ __forceinline__ unsigned cvt_pk_bf16(float lo, float hi) {
    typedef __bf16 b2 __attribute__((ext_vector_type(2)));
    b2 r = {(__bf16)lo, (__bf16)hi};
    return __builtin_bit_cast(unsigned, r);
}
__device__ __forceinline__ float dot2(unsigned x, unsigned y, float c) {
    typedef __bf16 b2 __attribute__((ext_vector_type(2)));
    return __builtin_amdgcn_fdot2_f32_bf16(__builtin_bit_cast(b2, x), __builtin_bit_cast(b2, y), c, false);
}
__device__ __forceinline__ f32x4 mfma16(v4u a, v4u b, f32x4 c) {
    return __builtin_amdgcn_mfma_f32_16x16x32_bf16(__builtin_bit_cast(bf16x8, a), __builtin_bit_cast(bf16x8, b), c, 0, 0, 0);
}
__device__ __forceinline__ float xor8_sum(float x) { return x + __builtin_bit_cast(float, __builtin_amdgcn_update_dpp(0, __builtin_bit_cast(int, x), 0x128, 0xf, 0xf, true)); }
__device__ __forceinline__ float xor16_max(float x) { auto s = __builtin_amdgcn_permlane16_swap(__float_as_uint(x), __float_as_uint(x), false, false); return fmaxf(__uint_as_float(s[0]), __uint_as_float(s[1])); }
__device__ __forceinline__ float xor32_max(float x) { auto s = __builtin_amdgcn_permlane32_swap(__float_as_uint(x), __float_as_uint(x), false, false); return fmaxf(__uint_as_float(s[0]), __uint_as_float(s[1])); }
__device__ __forceinline__ float xor16_sum(float x) { auto s = __builtin_amdgcn_permlane16_swap(__float_as_uint(x), __float_as_uint(x), false, false); return __uint_as_float(s[0]) + __uint_as_float(s[1]); }
__device__ __forceinline__ float xor32_sum(float x) { auto s = __builtin_amdgcn_permlane32_swap(__float_as_uint(x), __float_as_uint(x), false, false); return __uint_as_float(s[0]) + __uint_as_float(s[1]); }

__device__ __forceinline__ void lds_st(unsigned addr, unsigned v) { asm volatile("ds_write_b32 %0, %1" : : "v"(addr), "v"(v) : "memory"); }
__device__ __forceinline__ unsigned lds_ld(unsigned addr) { unsigned v; asm volatile("ds_read_b32 %0, %1\n\ts_waitcnt lgkmcnt(0)" : "=&v"(v) : "v"(addr) : "memory"); return __builtin_amdgcn_readfirstlane(v); }
__device__ __forceinline__ void glds3(unsigned voff, unsigned long long sbase, unsigned ldsdst) {
    unsigned keep;
    asm volatile("s_mov_b32 %0, m0\n\ts_mov_b32 m0, %3\n\ts_nop 0\n\t"
                 "global_load_lds_dwordx4 %1, %2 nt\n\t"
                 "global_load_lds_dwordx4 %1, %2 offset:1024 nt\n\t"
                 "global_load_lds_dwordx4 %1, %2 offset:2048 nt\n\t"
                 "s_mov_b32 m0, %0"
                 : "=&s"(keep) : "v"(voff), "s"(sbase), "s"(ldsdst) : "memory");
}

struct QArgs { const unsigned short* x[2]; const unsigned short* wq[2]; unsigned char* ws; };
__global__ void __launch_bounds__(512) k_qproj(QArgs a) {
    __shared__ float red[8][64];
    __shared__ __attribute__((aligned(16))) unsigned short xs[DM];
    const int tid = threadIdx.x;
    unsigned char* ws = a.ws;
    if (blockIdx.x >= 192) {
        const int e = blockIdx.x - 192;
        if (e == 0 && tid < NSHARD + NREP) *(unsigned*)(ws + WS_CTR + tid * CTR_STRIDE) = 0u;
        const v4u sent = {SENT, SENT, SENT, SENT};
        const int i = e * 512 + tid;
        for (int k = i; k < NWG * DM / 4; k += 64 * 512) *(v4u*)(ws + WS_OP + (size_t)k * 16) = sent;
        if (i < NTAIL / 2) *(v4u*)(ws + WS_PML + (size_t)NSH * 128 * 8 + (size_t)i * 16) = sent;
        for (int k = i; k < NTAIL * DH / 8; k += 64 * 512) *(v4u*)(ws + WS_PO + (size_t)NSH * 128 * DH * 2 + (size_t)k * 16) = sent;
        return;
    }
    const int b = blockIdx.x / 96, cb = blockIdx.x % 96;
    const unsigned short* wq = b ? a.wq[1] : a.wq[0];
    const int r0 = (tid >> 6) * 80 + ((tid >> 3) & 7), c8 = tid & 7;
    v4u w[10];
#pragma unroll
    for (int i = 0; i < 10; ++i) w[i] = __builtin_nontemporal_load((const GAS v4u*)(wq + (size_t)(r0 + 8 * i) * HD + cb * 64 + c8 * 8));
    const v4u xv4 = ((const GAS v4u*)(b ? a.x[1] : a.x[0]))[tid < DM / 8 ? tid : 0];
    if (tid < DM / 8) *(v4u*)(xs + tid * 8) = xv4;
    __syncthreads();
    float q8[8] = {0.f, 0.f, 0.f, 0.f, 0.f, 0.f, 0.f, 0.f};
#pragma unroll
    for (int i = 0; i < 10; ++i) { const float xv = bflo(xs[r0 + 8 * i]);
#pragma unroll
        for (int k = 0; k < 4; ++k) { q8[2 * k] = fmaf(xv, bflo(w[i][k]), q8[2 * k]); q8[2 * k + 1] = fmaf(xv, bfhi(w[i][k]), q8[2 * k + 1]); } }
#pragma unroll
    for (int k = 0; k < 8; ++k) q8[k] = xor32_sum(xor16_sum(xor8_sum(q8[k])));
    if ((tid & 63) < 8) {
#pragma unroll
        for (int k = 0; k < 8; ++k) red[tid >> 6][c8 * 8 + k] = q8[k];
    }
    __syncthreads();
    if (tid < 64) ((__bf16*)(ws + WS_Q))[b * HD + cb * 64 + tid] = (__bf16)(((red[0][tid] + red[1][tid]) + (red[2][tid] + red[3][tid])) + ((red[4][tid] + red[5][tid]) + (red[6][tid] + red[7][tid])));
}

struct StreamArgs { const unsigned char* k[2]; const unsigned char* v[2]; const unsigned short* wo[2]; unsigned char* ws; __bf16* out; };

__global__ void __launch_bounds__(NWAVE * 64, 1) k_stream(StreamArgs a) {
    extern __shared__ __attribute__((aligned(16))) unsigned char lds[];
    const int tid = threadIdx.x, lane = tid & 63, wave = __builtin_amdgcn_readfirstlane(tid >> 6);
    const int wg = blockIdx.x;
    const unsigned lds0 = (unsigned)(uintptr_t)lds;
    if (tid < 64) lds_st(lds0 + FULL_OFF + tid * 4, 0u);
    asm volatile("s_waitcnt lgkmcnt(0)\n\ts_barrier" ::: "memory");
    auto wait_gt = [&](unsigned addr, unsigned x, unsigned& seen) {
        if (seen > x) return;
        for (unsigned sp = 0; (seen = lds_ld(addr)) <= x && sp < SPIN_CAP; ++sp) __builtin_amdgcn_s_sleep(1);
    };
    const __amdgpu_buffer_rsrc_t rs = __builtin_amdgcn_make_buffer_rsrc((void*)a.ws, 0, (int)WS_END, 0x00020000);
    constexpr int SC1 = 16;
    GAS unsigned char* arr = (GAS unsigned char*)(a.ws + WS_CTR + NSHARD * CTR_STRIDE);
    auto put_record = [&](int r, int e) {
        const unsigned pidx = __float_as_uint(*(const LAS float*)(lds + PART_OFF + r * PREC + DH * 2 + 8));
        v4u v = *(const LAS v4u*)(lds + PART_OFF + r * PREC + e * 16);
#pragma unroll
        for (int k = 0; k < 4; ++k) v[k] = v[k] == SENT ? 0x7FC07FC0u : v[k];
        if (e < NPC - 1) __builtin_amdgcn_raw_buffer_store_b128(v, rs, (int)WS_PO + pidx * (DH * 2) + e * 16, 0, SC1);
        else __builtin_amdgcn_raw_buffer_store_b64(v2u{v[0], v[1]}, rs, (int)WS_PML + pidx * 8, 0, SC1);
    };
    const int tw = tid - 64, rg2 = (tw >= 80 && tw < 160) ? 1 : 0, cc = tw >= 0 && tw < 160 ? tw % 80 : 0;
    v4u wo_r[24];
    auto load_wo = [&]() {
        const unsigned short* wo = ((wg >> 7) ? a.wo[1] : a.wo[0]) + (size_t)(((wg >> 2) & 31) * DH + (wg & 3) * 48) * DM;
#pragma unroll
        for (int i = 0; i < 24; ++i) wo_r[i] = __builtin_nontemporal_load((const GAS v4u*)(wo + (size_t)(rg2 * 24 + i) * DM + cc * 8));
    };
    auto pack_wo = [&]() {
#pragma unroll
        for (int p = 0; p < 12; ++p)
#pragma unroll
            for (int k = 0; k < 4; ++k) { const unsigned r0 = wo_r[2 * p][k], r1 = wo_r[2 * p + 1][k];
                wo_r[2 * p][k] = __builtin_amdgcn_perm(r1, r0, 0x05040100u); wo_r[2 * p + 1][k] = __builtin_amdgcn_perm(r1, r0, 0x07060302u); }
    };
    if (wave >= 1 && wave <= 3) load_wo();
    const int skey = (wg & 127) * 256;

    if (wave == 0) {
#pragma unroll
        for (int i = 0; i < 24; ++i) wo_r[i] = v4u{0u, 0u, 0u, 0u};
        const unsigned voff = lane * 16;
        unsigned pub = 0, slot = 0, f = 0, tkseen = 0;
        auto fill = [&](unsigned long long src) {
            if (f >= NSLOT) {
                const unsigned need = f - NSLOT + 1;
                if (lds_ld(lds0 + FREE_OFF + slot * 4) < need) {
                    asm volatile("s_waitcnt vmcnt(0)" ::: "memory");
                    pub = f; lds_st(lds0 + FULL_OFF, pub);
                    for (unsigned sp = 0; lds_ld(lds0 + FREE_OFF + slot * 4) < need && sp < SPIN_CAP; ++sp) __builtin_amdgcn_s_sleep(1);
                }
            }
            const unsigned dst = lds0 + slot * SLOT_BYTES;
#pragma unroll
            for (int hf = 0; hf < 2; ++hf) {
                glds3(voff, src + hf * 6144, dst + hf * 6144);
                glds3(voff, src + hf * 6144 + 3072, dst + hf * 6144 + 3072);
                asm volatile("s_waitcnt vmcnt(%0)" ::"i"(INFL) : "memory");
                const int landed = (int)(f * 12 + hf * 6 + 6) - INFL;
                const unsigned np = landed > 0 ? (unsigned)landed / 12u : 0u;
                if (np > pub) { pub = np; lds_st(lds0 + FULL_OFF, pub); }
            }
            slot = (slot == NSLOT - 1) ? 0 : slot + 1;
            ++f;
        };
        for (int i = 0; i < NSTATIC; ++i) {
            const int br = i >= 2 * NJA, r = br ? i - 2 * NJA : i, nj = br ? NJB : NJA, kv = r >= nj;
            const int bh = 2 * ((br ? NJA : 0) + (kv ? r - nj : r)) + (wg >> 7);
            const unsigned char* base = kv ? (br ? a.v[1] : a.v[0]) : (br ? a.k[1] : a.k[0]);
            const unsigned long long src = (unsigned long long)(uintptr_t)base + ((size_t)(bh & 31) * SK + skey) * ROWB;
            if (i == NSTATIC - 1) lds_st(lds0 + LSTART_OFF, 1u);
            for (int t = 0; t < 8; ++t) fill(src + t * SLOT_BYTES);
        }
        for (unsigned n = 0;; ++n) {
            wait_gt(lds0 + TKCNT_OFF, n, tkseen);
            const unsigned t = lds_ld(lds0 + TK_OFF + (n & 7) * 4);
            if (t >= (unsigned)NTAIL) break;
            lds_st(lds0 + LSTART_OFF, n + 2);
            const int bh = NSH + t / NTB;
            const size_t off = ((size_t)(bh & 31) * SK + (t % NTB) * TKEYS) * ROWB;
            const unsigned long long kb = (unsigned long long)(uintptr_t)a.k[1] + off, vb = (unsigned long long)(uintptr_t)a.v[1] + off;
            for (int j = 0; j < 2 * TSL; ++j) fill((j >= TSL ? vb + (size_t)(j - TSL) * SLOT_BYTES : kb + (size_t)j * SLOT_BYTES));
        }
        asm volatile("s_waitcnt vmcnt(0)" ::: "memory");
        lds_st(lds0 + FULL_OFF, f);
    } else if (wave == 1) {
        pack_wo();
        const int shard = (wg >> 3) % NSHARD;
        GAS unsigned* ctr = (GAS unsigned*)(a.ws + WS_CTR + shard * CTR_STRIDE);
        unsigned n = 0, started = 0;
        int pubstate = 0;
        auto publish_static = [&]() {
            for (int i = lane; i < NJ * NPC; i += 64) put_record(i / NPC, i % NPC);
            pubstate = 1;
        };
        auto signal_static = [&]() {
            asm volatile("s_waitcnt vmcnt(0)" ::: "memory");
            if (lane < NREP) __hip_atomic_fetch_add((GAS unsigned*)(arr + lane * CTR_STRIDE), 1u, __ATOMIC_RELAXED, __HIP_MEMORY_SCOPE_AGENT);
            pubstate = 2;
        };
        for (; n < (unsigned)CAPT; ++n) {
            const unsigned need = n + 1 >= (unsigned)DEPTH ? n + 1 - DEPTH : 0;
            for (unsigned sp = 0; started <= need && (started = lds_ld(lds0 + LSTART_OFF)) <= need && sp < (SPIN_CAP << 4); ++sp) {
                if (pubstate == 0 && lds_ld(lds0 + SCNT_OFF) == (unsigned)NCONS) publish_static();
                __builtin_amdgcn_s_sleep(8);
            }
            unsigned j = 0;
            if (lane == 0) j = __hip_atomic_fetch_add(ctr, 1u, __ATOMIC_RELAXED, __HIP_MEMORY_SCOPE_AGENT);
            j = __builtin_amdgcn_readfirstlane(j);
            const unsigned t = j * NSHARD + shard;
            if (t < (unsigned)NTAIL) {
                lds_st(lds0 + TK_OFF + (n & 7) * 4, t);
                asm volatile("s_waitcnt lgkmcnt(0)" ::: "memory");
                lds_st(lds0 + TKCNT_OFF, n + 1);
            }
            if (pubstate == 1) signal_static();
            if (t >= (unsigned)NTAIL) break;
        }
        if (n >= 1) wait_gt(lds0 + LSTART_OFF, n >= 2 ? n - 1 : 0, started);
        lds_st(lds0 + NDONE_OFF, n);
#pragma unroll
        for (int i = 0; i < NCONS; ++i) lds_st(lds0 + TK_OFF + ((n + i) & 7) * 4, (unsigned)NTAIL);
        asm volatile("s_waitcnt lgkmcnt(0)" ::: "memory");
        lds_st(lds0 + TKCNT_OFF, n + NCONS);
        if (pubstate == 0) {
            for (unsigned sp = 0; lds_ld(lds0 + SCNT_OFF) != (unsigned)NCONS && sp < (SPIN_CAP << 4); ++sp) __builtin_amdgcn_s_sleep(2);
            publish_static();
        }
        if (pubstate == 1) signal_static();
        {   GAS unsigned* mine = (GAS unsigned*)(arr + (wg % NREP) * CTR_STRIDE);
            for (unsigned sp = 0; __hip_atomic_load(mine, __ATOMIC_RELAXED, __HIP_MEMORY_SCOPE_AGENT) < (unsigned)NWG && sp < SPIN_CAP; ++sp) __builtin_amdgcn_s_sleep(2);
            __builtin_amdgcn_fence(__ATOMIC_ACQUIRE, "wavefront"); }
    } else {
        const int cw = wave - 2;
        const int g = lane >> 4, c = lane & 15;
        const unsigned kaddr = c * ROWB + g * 16;
        const unsigned vaddr = (4 * g + (c >> 2)) * ROWB + (c & 3) * 8;
        unsigned fullseen = 0, tkseen = 0;
        auto load_q = [&](v4u (&q)[6], int bh) {
#pragma unroll
            for (int i = 0; i < 6; ++i) q[i] = *(const GAS v4u*)(a.ws + WS_Q + (size_t)bh * ROWB + i * 64 + g * 16);
        };
        auto qk_slot = [&](unsigned f, const v4u (&q)[6], f32x4& x0, f32x4& x1) {
            const unsigned slot = f % NSLOT;
            wait_gt(lds0 + FULL_OFF, f, fullseen);
            const LAS unsigned char* kp = (const LAS unsigned char*)(lds + slot * SLOT_BYTES) + kaddr;
            v4u kf[12];
#pragma unroll
            for (int i = 0; i < 12; ++i) kf[i] = *(const LAS v4u*)(kp + (i / 6) * 6144 + (i % 6) * 64);
            asm volatile("s_waitcnt lgkmcnt(0)" ::: "memory");
            lds_st(lds0 + FREE_OFF + slot * 4, f + 1);
#pragma unroll
            for (int i = 0; i < 12; ++i) asm volatile("" : "+v"(kf[i]));
            x0 = f32x4{0.f, 0.f, 0.f, 0.f}; x1 = x0;
#pragma unroll
            for (int i = 0; i < 6; ++i) { x0 = mfma16(kf[i], q[i], x0); x1 = mfma16(kf[6 + i], q[i], x1); }
        };
        auto pv_slot = [&](unsigned f, v4u P, f32x4 (&acc)[12]) {
            const unsigned slot = f % NSLOT;
            wait_gt(lds0 + FULL_OFF, f, fullseen);
            const LAS unsigned char* vp = (const LAS unsigned char*)(lds + slot * SLOT_BYTES) + vaddr;
            v2u vf[24];
#pragma unroll
            for (int i = 0; i < 24; ++i)
                vf[i] = __builtin_bit_cast(v2u, __builtin_amdgcn_ds_read_tr16_b64_v4i16((LAS s16x4*)(vp + (i & 1) * 6144 + (i >> 1) * 32)));
            asm volatile("s_waitcnt lgkmcnt(0)" ::: "memory");
            lds_st(lds0 + FREE_OFF + slot * 4, f + 1);
#pragma unroll
            for (int i = 0; i < 24; ++i) asm volatile("" : "+v"(vf[i]));
#pragma unroll
            for (int b = 0; b < 12; ++b) acc[b] = mfma16(v4u{vf[2 * b].x, vf[2 * b].y, vf[2 * b + 1].x, vf[2 * b + 1].y}, P, acc[b]);
        };
        auto softmax = [&](auto& S, auto& P, float& m, float& l) {
            constexpr int NS = sizeof(P) / sizeof(v4u);
            float mx = S[0][0];
#pragma unroll
            for (int tt = 0; tt < 2 * NS; ++tt)
#pragma unroll
                for (int r = 0; r < 4; ++r) mx = fmaxf(mx, S[tt][r]);
            m = xor32_max(xor16_max(mx)) * C2;
            float ls = 0.f;
#pragma unroll
            for (int tt = 0; tt < NS; ++tt) {
                float p[8];
#pragma unroll
                for (int r = 0; r < 8; ++r) { p[r] = __builtin_amdgcn_exp2f(fmaf(S[2 * tt + (r >> 2)][r & 3], C2, -m)); ls += p[r]; }
                P[tt] = v4u{cvt_pk_bf16(p[0], p[1]), cvt_pk_bf16(p[2], p[3]), cvt_pk_bf16(p[4], p[5]), cvt_pk_bf16(p[6], p[7])};
            }
            l = xor32_sum(xor16_sum(ls));
        };
        auto store_o = [&](int r, const f32x4 (&acc)[12]) {
            if (c == 0) {
#pragma unroll
                for (int b = 0; b < 12; ++b) *(LAS v2u*)((LAS unsigned char*)(lds + PART_OFF + r * PREC) + b * 32 + g * 8) = v2u{cvt_pk_bf16(acc[b][0], acc[b][1]), cvt_pk_bf16(acc[b][2], acc[b][3])};
            }
        };
        auto store_ml = [&](int r, float m, float l, unsigned pidx) {
            if (lane == 0) *(LAS f32x4*)((LAS unsigned char*)(lds + PART_OFF + r * PREC) + DH * 2) = f32x4{m, l, __uint_as_float(pidx), 0.f};
        };
#pragma unroll 1
        for (int br = 0; br < 2; ++br) {
            const int j0 = br ? NJA : 0, nj = br ? NJB : NJA, i0 = br ? 2 * NJA : 0;
            const int jf = j0 + ((cw - j0 % NCONS + NCONS) % NCONS);
            if (br == 1) pack_wo();
            v4u qn[6];
            load_q(qn, 2 * (jf < j0 + nj ? jf : j0) + (wg >> 7));
#pragma unroll 1
            for (int j = jf; j < j0 + nj; j += NCONS) {
                v4u q[6];
#pragma unroll
                for (int i = 0; i < 6; ++i) q[i] = qn[i];
                load_q(qn, 2 * (j + NCONS < j0 + nj ? j + NCONS : j) + (wg >> 7));
                const unsigned f0 = (unsigned)(i0 + j - j0) * 8;
                f32x4 S[16];
#pragma unroll
                for (int tt = 0; tt < 8; ++tt) qk_slot(f0 + tt, q, S[2 * tt], S[2 * tt + 1]);
                v4u P[8]; float m, l;
                softmax(S, P, m, l);
                if (c == 0) {
#pragma unroll
                    for (int tt = 0; tt < 8; ++tt) *(LAS v4u*)((LAS unsigned char*)(lds + P_OFF + (j - j0) * 512) + tt * 64 + g * 16) = P[tt];
                }
                store_ml(j, m, l, (unsigned)((2 * j + (wg >> 7)) * 128 + (wg & 127)));
            }
#pragma unroll 1
            for (int j = jf; j < j0 + nj; j += NCONS) {
                const unsigned f0 = (unsigned)(i0 + nj + j - j0) * 8;
                f32x4 acc[12];
#pragma unroll
                for (int b = 0; b < 12; ++b) acc[b] = f32x4{0.f, 0.f, 0.f, 0.f};
#pragma unroll
                for (int tt = 0; tt < 8; ++tt) {
                    const v4u P = *(const LAS v4u*)((const LAS unsigned char*)(lds + P_OFF + (j - j0) * 512) + tt * 64 + g * 16);
                    pv_slot(f0 + tt, P, acc);
                }
                store_o(j, acc);
            }
        }
        asm volatile("s_waitcnt lgkmcnt(0)" ::: "memory");
        if (lane == 0) __hip_atomic_fetch_add((LAS unsigned*)(lds + SCNT_OFF), 1u, __ATOMIC_RELAXED, __HIP_MEMORY_SCOPE_WORKGROUP);
#pragma unroll 1
        for (unsigned n = cw;; n += NCONS) {
            wait_gt(lds0 + TKCNT_OFF, n, tkseen);
            const unsigned t = lds_ld(lds0 + TK_OFF + (n & 7) * 4);
            if (t >= (unsigned)NTAIL) break;
            v4u q[6];
            load_q(q, NSH + t / NTB);
            const unsigned f0 = NSTATIC * 8 + n * (2 * TSL);
            f32x4 S[2 * TSL];
#pragma unroll
            for (int tt = 0; tt < TSL; ++tt) qk_slot(f0 + tt, q, S[2 * tt], S[2 * tt + 1]);
            v4u P[TSL]; float m, l;
            softmax(S, P, m, l);
            f32x4 acc[12];
#pragma unroll
            for (int b = 0; b < 12; ++b) acc[b] = f32x4{0.f, 0.f, 0.f, 0.f};
#pragma unroll
            for (int tt = 0; tt < TSL; ++tt) pv_slot(f0 + TSL + tt, P[tt], acc);
            store_o(NJ + cw, acc);
            store_ml(NJ + cw, m, l, NSH * 128 + t);
            if (lane < NPC) put_record(NJ + cw, lane);
        }
    }
    asm volatile("s_waitcnt lgkmcnt(0)\n\ts_barrier" ::: "memory");
    {
        LAS float* red = (LAS float*)lds;
        LAS float* os = red + 32 * 52;
        LAS float* ored = os + 3 * 24;
        const int bh = wg >> 2, js = wg & 3;
        const int rg = (tid >> 3) & 31, c8 = tid & 7;
        constexpr int RT = NTB / 32;
        const int row0 = (bh < NSH ? bh * 128 : NSH * 128 + (bh - NSH) * NTB) + rg;
        const int prow = (int)WS_PO + row0 * (DH * 2) + js * 96 + (c8 < 6 ? c8 : 0) * 16, pml = (int)WS_PML + row0 * 8;
        v4u po[RT]; v2u ml[RT];
        auto has_data = [&](int i) { return po[i][0] != SENT && po[i][1] != SENT && po[i][2] != SENT && po[i][3] != SENT && ml[i][0] != SENT && ml[i][1] != SENT; };
        if (bh < NSH) {
#pragma unroll
            for (int i = 0; i < 4; ++i) { po[i] = __builtin_amdgcn_raw_buffer_load_b128(rs, prow + 32 * i * (DH * 2), 0, SC1); ml[i] = __builtin_amdgcn_raw_buffer_load_b64(rs, pml + 32 * i * 8, 0, SC1); }
#pragma unroll
            for (int i = 4; i < RT; ++i) { po[i] = v4u{0u, 0u, 0u, 0u}; ml[i] = v2u{0xFF800000u, 0u}; }
        } else {
            for (unsigned sp = 0; sp < SPIN_CAP; ++sp) {
#pragma unroll
                for (int i = 0; i < RT; ++i) { po[i] = __builtin_amdgcn_raw_buffer_load_b128(rs, prow + 32 * i * (DH * 2), 0, SC1); ml[i] = __builtin_amdgcn_raw_buffer_load_b64(rs, pml + 32 * i * 8, 0, SC1); }
                bool ok = true;
#pragma unroll
                for (int i = 0; i < RT; ++i) ok = ok && has_data(i);
                if (__all(ok)) break;
                __builtin_amdgcn_s_sleep(1);
            }
        }
        float Mt = __uint_as_float(ml[0][0]);
#pragma unroll
        for (int i = 1; i < RT; ++i) Mt = fmaxf(Mt, __uint_as_float(ml[i][0]));
        float Lt = 0.f, t[8] = {0.f, 0.f, 0.f, 0.f, 0.f, 0.f, 0.f, 0.f};
#pragma unroll
        for (int i = 0; i < RT; ++i) {
            const float w_ = __builtin_amdgcn_exp2f(__uint_as_float(ml[i][0]) - Mt);
            Lt = fmaf(w_, __uint_as_float(ml[i][1]), Lt);
#pragma unroll
            for (int k = 0; k < 4; ++k) { t[2 * k] = fmaf(w_, bflo(po[i][k]), t[2 * k]); t[2 * k + 1] = fmaf(w_, bfhi(po[i][k]), t[2 * k + 1]); }
        }
        if (tid < 256) {
            if (c8 < 6) { *(LAS f32x4*)(red + rg * 52 + c8 * 8) = f32x4{t[0], t[1], t[2], t[3]}; *(LAS f32x4*)(red + rg * 52 + c8 * 8 + 4) = f32x4{t[4], t[5], t[6], t[7]}; }
            else if (c8 == 6) *(LAS f32x2*)(red + rg * 52 + 48) = f32x2{Mt, Lt};
        }
        __syncthreads();
        if (wave >= 1 && wave <= 3) {
            const float Mr = lane < 32 ? red[lane * 52 + 48] : -INFINITY, Lr = lane < 32 ? red[lane * 52 + 49] : 0.f;
            float M = Mr;
            M = fmaxf(M, __builtin_bit_cast(float, __builtin_amdgcn_update_dpp(0, __builtin_bit_cast(int, M), 0xB1, 0xf, 0xf, true)));
            M = fmaxf(M, __builtin_bit_cast(float, __builtin_amdgcn_update_dpp(0, __builtin_bit_cast(int, M), 0x4E, 0xf, 0xf, true)));
            M = fmaxf(M, __builtin_bit_cast(float, __builtin_amdgcn_update_dpp(0, __builtin_bit_cast(int, M), 0x141, 0xf, 0xf, true)));
            M = fmaxf(M, __builtin_bit_cast(float, __builtin_amdgcn_update_dpp(0, __builtin_bit_cast(int, M), 0x128, 0xf, 0xf, true)));
            M = xor32_max(xor16_max(M));
            const float fr = __builtin_amdgcn_exp2f(Mr - M);
            float L = 0.f, o = 0.f;
            const int d = lane < 48 ? lane : 0;
#pragma unroll
            for (int r = 0; r < 32; ++r) {
                const float f = __builtin_bit_cast(float, __builtin_amdgcn_readlane(__builtin_bit_cast(int, fr), r));
                L = fmaf(f, __builtin_bit_cast(float, __builtin_amdgcn_readlane(__builtin_bit_cast(int, Lr), r)), L);
                o = fmaf(f, red[r * 52 + d], o);
            }
            LAS unsigned short* osw = (LAS unsigned short*)(os + (wave - 1) * 24);
            if (lane < 48) osw[lane] = __builtin_bit_cast(unsigned short, (__bf16)(o / L));
            float acc[8] = {0.f, 0.f, 0.f, 0.f, 0.f, 0.f, 0.f, 0.f};
#pragma unroll
            for (int p = 0; p < 12; ++p) { const unsigned op = ((const LAS unsigned*)osw)[rg2 * 12 + p];
#pragma unroll
                for (int k = 0; k < 4; ++k) { acc[2 * k] = dot2(wo_r[2 * p][k], op, acc[2 * k]); acc[2 * k + 1] = dot2(wo_r[2 * p + 1][k], op, acc[2 * k + 1]); } }
            if (tw < 160) { *(LAS f32x4*)(ored + rg2 * DM + cc * 8) = f32x4{acc[0], acc[1], acc[2], acc[3]}; *(LAS f32x4*)(ored + rg2 * DM + cc * 8 + 4) = f32x4{acc[4], acc[5], acc[6], acc[7]}; }
        }
        __syncthreads();
        if (tid < DM / 4) {
            v4u v;
#pragma unroll
            for (int k = 0; k < 4; ++k) { const unsigned u = __float_as_uint(ored[tid * 4 + k] + ored[DM + tid * 4 + k]); v[k] = u == SENT ? 0x7FC00000u : u; }
            __builtin_amdgcn_raw_buffer_store_b128(v, rs, (int)WS_OP + wg * (DM * 4) + tid * 16, 0, SC1);
        }
    }
    if (wg < 40) {
        LAS float* red2 = (LAS float*)(lds + 16384);
        const int b = wg / 20, cb = wg % 20, rg = (tid >> 3) & 31, c4 = tid & 7;
        v4u v[4];
        for (unsigned sp = 0; sp < SPIN_CAP; ++sp) {
            bool ok = true;
#pragma unroll
            for (int i = 0; i < 4; ++i) { v[i] = __builtin_amdgcn_raw_buffer_load_b128(rs, (int)WS_OP + (b * 128 + rg + 32 * i) * (DM * 4) + cb * 128 + c4 * 16, 0, SC1);
                ok = ok && v[i][0] != SENT && v[i][1] != SENT && v[i][2] != SENT && v[i][3] != SENT; }
            if (__all(ok)) break;
            __builtin_amdgcn_s_sleep(2);
        }
        const f32x4 t = (__builtin_bit_cast(f32x4, v[0]) + __builtin_bit_cast(f32x4, v[1])) + (__builtin_bit_cast(f32x4, v[2]) + __builtin_bit_cast(f32x4, v[3]));
        if (tid < 256) {
#pragma unroll
            for (int k = 0; k < 4; ++k) red2[rg * 32 + c4 * 4 + k] = t[k];
        }
        __syncthreads();
        if (tid < 32) { float u = 0.f;
#pragma unroll
            for (int r = 0; r < 32; ++r) u += red2[r * 32 + tid];
            a.out[b * DM + cb * 32 + tid] = (__bf16)u; }
    }
}

extern "C" void kernel_launch(void* const* d_in, const int* in_sizes, int n_in, void* d_out, int out_size, void* d_ws, size_t ws_size, hipStream_t stream) {
    (void)in_sizes; (void)n_in; (void)out_size; (void)ws_size;
    unsigned char* ws = (unsigned char*)d_ws;
    (void)hipFuncSetAttribute((const void*)k_stream, hipFuncAttributeMaxDynamicSharedMemorySize, LDS_BYTES);
    QArgs qa;
    qa.x[0] = (const unsigned short*)d_in[0]; qa.x[1] = (const unsigned short*)d_in[5];
    qa.wq[0] = (const unsigned short*)d_in[3]; qa.wq[1] = (const unsigned short*)d_in[8];
    qa.ws = ws;
    hipLaunchKernelGGL(k_qproj, dim3(256), dim3(512), 0, stream, qa);
    StreamArgs sa;
    sa.k[0] = (const unsigned char*)d_in[1]; sa.k[1] = (const unsigned char*)d_in[6];
    sa.v[0] = (const unsigned char*)d_in[2]; sa.v[1] = (const unsigned char*)d_in[7];
    sa.wo[0] = (const unsigned short*)d_in[4]; sa.wo[1] = (const unsigned short*)d_in[9];
    sa.ws = ws; sa.out = (__bf16*)d_out;
    hipLaunchKernelGGL(k_stream, dim3(NWG), dim3(NWAVE * 64), LDS_BYTES, stream, sa);
}
```
